```python
import torch

def _softmax(x, axis=-1):
    return torch.softmax(x, dim=axis)

def _gelu(x):
    return torch.nn.functional.gelu(x, approximate='tanh')

def _silu(x):
    return torch.nn.functional.silu(x)

def _sigmoid(x):
    return torch.sigmoid(x)

def _layernorm(x, axis=-1, eps=1e-05):
    m = x.mean(dim=axis, keepdim=True)
    v = x.var(dim=axis, keepdim=True, unbiased=False)
    return (x - m) / torch.sqrt(v + eps)

def _scatter_add(tgt, idx, src, axis=0):
    return tgt.clone().index_add_(axis, idx, src)

def _segment_sum(src, seg, n):
    out = torch.zeros((n,) + src.shape[1:], dtype=src.dtype)
    return out.index_add_(0, seg, src)

def _pad(x, pw):
    flat = tuple((v for pair in reversed(pw) for v in pair))
    return torch.nn.functional.pad(x, flat)
kernel_name = 'adaLN_cond__gqa_block_cached__glu_gated_geglu__decode'

def setup_inputs():

    _w = lambda a: (lambda t: t.to(torch.bfloat16) if t.is_floating_point() else t)(torch.as_tensor(a))

    def _A_setup():
        B, Sq, D = (2, 1, 512)
        Hq, Hkv, Sk, Dh = (32, 8, 32768, 128)
        assert Hq % Hkv == 0, (Hq, Hkv)
        return {'x': _w((lambda *s: torch.randn(*s, dtype=torch.bfloat16) * 0.05)(B, Sq, D)), 'k_cache': _w((lambda *s: torch.randn(*s, dtype=torch.bfloat16) * 0.8)(B, Hkv, Sk, Dh)), 'v_cache': _w((lambda *s: torch.randn(*s, dtype=torch.bfloat16) * 3.2)(B, Hkv, Sk, Dh)), 'wq': _w((lambda *s: torch.randn(*s, dtype=torch.bfloat16) * 0.8)(D, Hq * Dh) * D ** (-0.5)), 'wo': _w((lambda *s: torch.randn(*s, dtype=torch.bfloat16) * 3.2)(Hq * Dh, D) * (Hq * Dh) ** (-0.5))}
    _A_in = _A_setup()

    def _B_setup():
        B, S, Dm, Df = (2, 1, 512, 1792)
        return {'x': _w((lambda *s: torch.randn(*s, dtype=torch.bfloat16) * 0.05)(B, S, Dm)), 'w_gate': _w((lambda *s: torch.randn(*s, dtype=torch.bfloat16) * 3.2)(Dm, Df) * Dm ** (-0.5)), 'w_up': _w((lambda *s: torch.randn(*s, dtype=torch.bfloat16) * 3.2)(Dm, Df) * Dm ** (-0.5)), 'w_down': _w((lambda *s: torch.randn(*s, dtype=torch.bfloat16) * 3.2)(Df, Dm) * Df ** (-0.5))}
    _B_in = _B_setup()
    return {'A_x': _A_in['x'], 'A_k_cache': _A_in['k_cache'], 'A_v_cache': _A_in['v_cache'], 'A_wq': _A_in['wq'], 'A_wo': _A_in['wo'], 'B_x': _B_in['x'], 'B_w_gate': _B_in['w_gate'], 'B_w_up': _B_in['w_up'], 'B_w_down': _B_in['w_down'], 'c': _w((lambda *s: torch.randn(*s, dtype=torch.bfloat16) * 0.4)(2, 512) * 8.0), 'w_mod': _w((lambda *s: torch.randn(*s, dtype=torch.bfloat16) * 0.4)(512, 6 * 512) * 512 ** (-0.5) * 8.0)}

def reference(A_x, A_k_cache, A_v_cache, A_wq, A_wo, B_x, B_w_gate, B_w_up, B_w_down, c, w_mod):

    def _A_ref(x, k_cache, v_cache, wq, wo):
        B, Sq, D = x.shape
        Hq, Dh = (32, 128)
        Hkv = k_cache.shape[1]
        rep = Hq // Hkv
        q = (lambda x, *d: x.permute(*d))((x @ wq).reshape(B, Sq, Hq, Dh), 0, 2, 1, 3)
        k = (lambda x, n, axis: torch.repeat_interleave(x, n, dim=axis))(k_cache, rep, axis=1)
        v = (lambda x, n, axis: torch.repeat_interleave(x, n, dim=axis))(v_cache, rep, axis=1)
        scores = torch.einsum('bhqd,bhkd->bhqk', q, k) * Dh ** (-0.5)
        p = _softmax(scores, axis=-1)
        o = torch.einsum('bhqk,bhkd->bhqd', p, v)
        return (lambda x, *d: x.permute(*d))(o, 0, 2, 1, 3).reshape(B, Sq, Hq * Dh) @ wo

    def _B_ref(x, w_gate, w_up, w_down):
        g = torch.einsum('bsd,df->bsf', x, w_gate)
        u = torch.einsum('bsd,df->bsf', x, w_up)
        h = _gelu(g) * u
        return torch.einsum('bsf,fd->bsd', h, w_down)
    x = A_x
    mod = (_silu(c) @ w_mod).reshape(c.shape[0], 6, 1, -1)
    sa, ca, ga, sb, cb, gb = (mod[:, 0].clone(), mod[:, 1].clone(), mod[:, 2].clone(), mod[:, 3].clone(), mod[:, 4].clone(), mod[:, 5].clone())
    h = _layernorm(x, axis=-1) * (1 + ca) + sa
    x = x + ga * _A_ref(h, A_k_cache, A_v_cache, A_wq, A_wo)
    h = _layernorm(x, axis=-1) * (1 + cb) + sb
    x = x + gb * _B_ref(h, B_w_gate, B_w_up, B_w_down)
    return x
```

```cpp
#include <hip/hip_runtime.h>
#include <hip/hip_bf16.h>

typedef unsigned u32;
typedef unsigned short u16;
typedef unsigned long long u64;
typedef u32 u32x4 __attribute__((ext_vector_type(4)));
typedef u32 u32x2 __attribute__((ext_vector_type(2)));
typedef float f32x4 __attribute__((ext_vector_type(4)));
typedef float f32x2 __attribute__((ext_vector_type(2)));
typedef short s16x4 __attribute__((ext_vector_type(4)));
typedef __bf16 bf16x8 __attribute__((ext_vector_type(8)));
typedef __bf16 bf16x2 __attribute__((ext_vector_type(2)));
typedef __attribute__((address_space(1))) u64 gu64;
typedef __attribute__((address_space(1))) u32 gu32;
typedef __attribute__((address_space(3))) u32 lu32;

#define RLX_AGENT __ATOMIC_RELAXED, __HIP_MEMORY_SCOPE_AGENT
#define DEVI __device__ __forceinline__

#define NPRE 5
#define STAMPS 0

constexpr int D = 512, SK = 32768, DF = 1792;
constexpr int NWG = 256, NTHR = 512;
constexpr int TILES = 32;
constexpr float SCL = 0.08838834764831845f * 1.4426950408889634f;
constexpr u32 SPIN_CAP = 1u << 18;

constexpr size_t WS_EPOCH = 0;
constexpr size_t WS_H   = 4096;
constexpr size_t WS_MX  = WS_H + 4096;
constexpr size_t WS_X2  = WS_MX + 16384;
constexpr size_t WS_Q   = WS_X2 + 4096;
constexpr size_t WS_P1L = WS_Q + 32768;
constexpr size_t WS_P1O = WS_P1L + 32768;
constexpr size_t WS_P2  = WS_P1O + 256 * 4096;
constexpr size_t WS_P4  = WS_P2 + 256 * 8192;
constexpr size_t WS_ST  = WS_P4 + 256 * 8192;

struct Params {
  const u16 *x, *k, *v, *wq, *wo, *wg, *wu, *wd, *c, *wmod;
  u16* out;
  char* ws;
};

DEVI float bf_lo(u32 w) { return __uint_as_float(w << 16); }
DEVI float bf_hi(u32 w) { return __uint_as_float(w & 0xffff0000u); }
DEVI u32 pk_bf(float lo, float hi) {
  bf16x2 r = {(__bf16)lo, (__bf16)hi};
  return __builtin_bit_cast(u32, r);
}
DEVI float rbf(float x) { return (float)(__bf16)x; }
DEVI float dot2(u32 a, u32 b, float c) {
  return __builtin_amdgcn_fdot2_f32_bf16(__builtin_bit_cast(bf16x2, a), __builtin_bit_cast(bf16x2, b), c, false);
}
DEVI u32 perm_lo(u32 a, u32 b) { return __builtin_amdgcn_perm(b, a, 0x05040100u); }
DEVI u32 perm_hi(u32 a, u32 b) { return __builtin_amdgcn_perm(b, a, 0x07060302u); }
template <int CTRL> DEVI float dpp(float x) {
  return __builtin_bit_cast(float, __builtin_amdgcn_mov_dpp(__builtin_bit_cast(int, x), CTRL, 0xf, 0xf, true));
}
constexpr int XOR1 = 0xB1, XOR2 = 0x4E, XOR7 = 0x141, ROR8 = 0x128;
DEVI float fold32(float a, float b) {
  auto s = __builtin_amdgcn_permlane32_swap(__float_as_uint(a), __float_as_uint(b), false, false);
  return __uint_as_float(s[0]) + __uint_as_float(s[1]);
}
DEVI float fold16(float a, float b) {
  auto s = __builtin_amdgcn_permlane16_swap(__float_as_uint(a), __float_as_uint(b), false, false);
  return __uint_as_float(s[0]) + __uint_as_float(s[1]);
}
DEVI float max32(float a) {
  auto s = __builtin_amdgcn_permlane32_swap(__float_as_uint(a), __float_as_uint(a), false, false);
  return fmaxf(__uint_as_float(s[0]), __uint_as_float(s[1]));
}
DEVI float max16(float a) {
  auto s = __builtin_amdgcn_permlane16_swap(__float_as_uint(a), __float_as_uint(a), false, false);
  return fmaxf(__uint_as_float(s[0]), __uint_as_float(s[1]));
}
DEVI float row_sum(float x) { x += dpp<XOR1>(x); x += dpp<XOR2>(x); x += dpp<XOR7>(x); x += dpp<ROR8>(x); return x; }
DEVI float wave_sum(float x) { x = row_sum(x); x = fold16(x, x); return fold32(x, x); }
DEVI u32x4 ldnt(const void* base, u32 off) { return __builtin_nontemporal_load((const u32x4*)((const char*)base + off)); }
DEVI u32 ldnt1(const void* base, u32 off) { return __builtin_nontemporal_load((const u32*)((const char*)base + off)); }
DEVI void put(gu64* g, u32 tag, u32 val) { __hip_atomic_store(g, ((u64)tag << 32) | val, RLX_AGENT); }
DEVI void putf(gu64* g, u32 tag, float val) { put(g, tag, __float_as_uint(val)); }
template <int N, typename F> DEVI void gather(F addr, u32 tag, u32 (&v)[N], bool active = true) {
#pragma unroll 1
  for (u32 spins = 0; spins < SPIN_CAP; ++spins) {
    bool ok = true;
    u64 x[N];
#pragma unroll
    for (int i = 0; i < N; ++i) x[i] = __hip_atomic_load(addr(i), RLX_AGENT);
#pragma unroll
    for (int i = 0; i < N; ++i) { v[i] = (u32)x[i]; ok &= (u32)(x[i] >> 32) == tag; }
    if (__builtin_amdgcn_ballot_w64(!ok && active) == 0) break;
    __builtin_amdgcn_s_sleep(1);
  }
}
DEVI void lds_wait_lgkm() { asm volatile("s_waitcnt lgkmcnt(0)" ::: "memory"); }
DEVI void wg_barrier() { asm volatile("s_waitcnt lgkmcnt(0)\n\ts_barrier" ::: "memory"); }
DEVI void lds_arrive(u32* cnt, int lane) {
  lds_wait_lgkm();
  if (lane == 0) __hip_atomic_fetch_add(cnt, 1u, __ATOMIC_RELAXED, __HIP_MEMORY_SCOPE_WORKGROUP);
}
DEVI void lds_wait(u32* cnt, u32 want) {
#pragma unroll 1
  for (u32 spins = 0; spins < (SPIN_CAP << 6); ++spins) {
    if (__builtin_amdgcn_readfirstlane(__hip_atomic_load(cnt, __ATOMIC_RELAXED, __HIP_MEMORY_SCOPE_WORKGROUP)) >= want) break;
    __builtin_amdgcn_s_sleep(1);
  }
  asm volatile("" ::: "memory");
}
#define STAMP(i) do {} while (0)

DEVI void narrow_pack(const u32x4 (&r)[4], u32 (&w)[16]) {
#pragma unroll
  for (int m = 0; m < 2; ++m)
#pragma unroll
    for (int i = 0; i < 4; ++i) { w[m * 8 + 2 * i] = perm_lo(r[2 * m][i], r[2 * m + 1][i]); w[m * 8 + 2 * i + 1] = perm_hi(r[2 * m][i], r[2 * m + 1][i]); }
}
DEVI float narrow_dot(const u32 (&w)[16], u32 a0, u32 a1, int lane) {
  float t[16];
#pragma unroll
  for (int i = 0; i < 16; ++i) t[i] = fold32(dot2(w[i], a0, 0.f), dot2(w[i], a1, 0.f));
#pragma unroll
  for (int i = 0; i < 8; ++i) t[i] = fold16(t[i], t[i + 8]);
#pragma unroll
  for (int i = 0; i < 4; ++i) { const float a = t[i] + dpp<ROR8>(t[i]), b = t[i + 4] + dpp<ROR8>(t[i + 4]); t[i] = (lane & 8) ? b : a; }
#pragma unroll
  for (int i = 0; i < 2; ++i) { const float a = t[i] + dpp<XOR7>(t[i]), b = t[i + 2] + dpp<XOR7>(t[i + 2]); t[i] = (lane & 4) ? b : a; }
  { const float a = t[0] + dpp<XOR2>(t[0]), b = t[1] + dpp<XOR2>(t[1]); t[0] = (lane & 2) ? b : a; }
  return t[0] + dpp<XOR1>(t[0]);
}
DEVI float wave_sum4(float v0, float v1, float v2, float v3) { return row_sum(fold16(fold32(v0, v2), fold32(v1, v3))); }

struct Att { f32x4 acc[8]; float m, l; bf16x8 q; };

DEVI void ld_tile(u32x4 (&d)[4], const char* base, u32 off) {
#pragma unroll
  for (int i = 0; i < 4; ++i) d[i] = ldnt(base + i * 1024, off);
}
DEVI void qk(const Att& a, const u32x4 (&k)[4], f32x4 (&s)[4]) {
#pragma unroll
  for (int i = 0; i < 4; ++i)
    s[i] = __builtin_amdgcn_mfma_f32_16x16x32_bf16(__builtin_bit_cast(bf16x8, k[i]), a.q, f32x4{0.f, 0.f, 0.f, 0.f}, 0, 0, 0);
}
DEVI s16x4 softmax_step(Att& a, const f32x4 (&d)[4]) {
  float s[4];
#pragma unroll
  for (int i = 0; i < 4; ++i) s[i] = (dpp<0x00>(d[i][0]) + dpp<0x55>(d[i][1])) + (dpp<0xAA>(d[i][2]) + dpp<0xFF>(d[i][3]));
  float t = fmaxf(fmaxf(s[0], s[1]), fmaxf(s[2], s[3]));
  t = max32(max16(t));
  if (__builtin_amdgcn_ballot_w64(t > a.m) != 0) {
    const float mn = fmaxf(a.m, t);
    const float al = __builtin_amdgcn_exp2f((a.m - mn) * SCL);
    a.m = mn; a.l *= al;
#pragma unroll
    for (int j = 0; j < 8; ++j) a.acc[j] *= al;
  }
  const float mc = a.m * SCL;
  float pr[4];
#pragma unroll
  for (int i = 0; i < 4; ++i) pr[i] = __builtin_amdgcn_exp2f(fmaf(s[i], SCL, -mc));
  a.l += (pr[0] + pr[1]) + (pr[2] + pr[3]);
  u32x2 pk = {pk_bf(pr[0], pr[1]), pk_bf(pr[2], pr[3])};
  return __builtin_bit_cast(s16x4, pk);
}
DEVI void vtrans(const u32x4 (&v)[4], u32x2 (&t)[8]) {
#pragma unroll
  for (int i = 0; i < 4; ++i) {
    t[2 * i]     = u32x2{perm_lo(v[0][i], v[1][i]), perm_lo(v[2][i], v[3][i])};
    t[2 * i + 1] = u32x2{perm_hi(v[0][i], v[1][i]), perm_hi(v[2][i], v[3][i])};
  }
}
DEVI void pv(Att& a, const u32x2 (&t)[8], s16x4 p) {
#pragma unroll
  for (int j = 0; j < 8; ++j) a.acc[j] = __builtin_amdgcn_mfma_f32_16x16x16bf16_1k(__builtin_bit_cast(s16x4, t[j]), p, a.acc[j], 0, 0, 0);
}
#define SB() __builtin_amdgcn_sched_barrier(0)
template <bool REFILL> DEVI void tile_step(Att& a, u32x4 (&k)[4], u32x4 (&v)[4], const char* kn, const char* vn, u32 offk, u32 offv) {
  f32x4 s[4]; u32x2 t[8];
  qk(a, k, s);
  SB();
  if (REFILL) ld_tile(k, kn, offk);
  SB();
  const s16x4 p = softmax_step(a, s);
  SB();
  vtrans(v, t);
  SB();
  if (REFILL) ld_tile(v, vn, offv);
  SB();
  pv(a, t, p);
  SB();
}
DEVI void touch(const u32x4 (&d)[4]) { asm volatile("" ::"v"(d[0]), "v"(d[1]), "v"(d[2]), "v"(d[3])); }

constexpr int L_CNT = 0;
constexpr int L_Q = 16;
constexpr int L_RED = L_Q + 256;
constexpr int L_RED2 = L_RED + 256;
constexpr int L_RED3 = L_RED2 + 256;
constexpr int L_ML = L_RED3 + 1024;
constexpr int L_O = L_ML + 32;
constexpr int L_END = L_O + 2048;

__global__ void __launch_bounds__(NTHR, 2) fused_block_kernel(Params p) {
  __shared__ __attribute__((aligned(16))) u32 lds[L_END];
  float* ldsf = (float*)lds;
  const int tid = threadIdx.x, lane = tid & 63;
  const int wave = __builtin_amdgcn_readfirstlane(tid >> 6);
  const int wg = blockIdx.x;
  const int pair = wg >> 4, sp = wg & 15, bb = pair >> 3, hkv = pair & 7;

  if (wave < 4) {
    STAMP(wave == 0 ? 0 : 63);
    const char* kb = (const char*)p.k + (size_t)wg * 524288 + wave * 4096;
    const char* vb = (const char*)p.v + (size_t)wg * 524288 + wave * 4096;
    const u32 offk = ((lane & 15) >> 2) * 256 + (lane >> 4) * 64 + (lane & 3) * 16, offv = lane * 16;
    u32x4 K[NPRE][4], V[NPRE][4];
    ld_tile(K[0], kb, offk);
    SB();
    wg_barrier();
    ld_tile(V[0], vb, offv);
    SB();
#pragma unroll
    for (int t = 1; t < NPRE; ++t) {
      touch(K[t - 1]); SB();
      ld_tile(K[t], kb + t * 16384, offk); SB();
      touch(V[t - 1]); SB();
      ld_tile(V[t], vb + t * 16384, offv); SB();
    }
    STAMP(wave == 0 ? 1 : 63);
    lds_wait(&lds[L_CNT + 2], 1);
    STAMP(wave == 0 ? 2 : 63);
    Att a;
    a.q = __builtin_bit_cast(bf16x8, *(const u32x4*)&lds[L_Q + lane * 4]);
    a.m = -INFINITY; a.l = 0.f;
#pragma unroll
    for (int j = 0; j < 8; ++j) a.acc[j] = f32x4{0.f, 0.f, 0.f, 0.f};
    tile_step<true>(a, K[0], V[0], kb + NPRE * 16384, vb + NPRE * 16384, offk, offv);
#pragma unroll
    for (int t = 1; t < NPRE; ++t) tile_step<false>(a, K[t], V[t], nullptr, nullptr, offk, offv);
#pragma unroll 1
    for (int t = NPRE + 1; t < TILES; ++t) tile_step<true>(a, K[0], V[0], kb + t * 16384, vb + t * 16384, offk, offv);
    tile_step<false>(a, K[0], V[0], nullptr, nullptr, offk, offv);
    STAMP(wave == 0 ? 3 : 63);
    const float lsum = fold32(fold16(a.l, a.l), fold16(a.l, a.l));
    const int qq = lane & 3, h = (lane & 15) >> 2, c = lane >> 4;
    float o[8];
#pragma unroll
    for (int j = 0; j < 8; ++j) { const float lo = (qq & 1) ? a.acc[j][1] : a.acc[j][0], hi = (qq & 1) ? a.acc[j][3] : a.acc[j][2]; o[j] = (qq & 2) ? hi : lo; }
    float* rec = ldsf + L_O + wave * 512 + h * 128 + (4 * c + qq) * 8;
    *(f32x4*)rec = f32x4{o[0], o[1], o[2], o[3]};
    *(f32x4*)(rec + 4) = f32x4{o[4], o[5], o[6], o[7]};
    if (lane < 16 && qq == 0) { ldsf[L_ML + wave * 8 + h] = a.m; ldsf[L_ML + wave * 8 + 4 + h] = lsum; }
    wg_barrier();
    return;
  }

  const int sw = wave - 4, u = tid - 256;
  const int xc = wg & 7, yc = wg >> 3;
  const int tcol = xc * 32 + yc;
  gu64* const gH = (gu64*)(p.ws + WS_H);
  gu64* const gMX = (gu64*)(p.ws + WS_MX);
  gu64* const gX2 = (gu64*)(p.ws + WS_X2);
  gu64* const gQ = (gu64*)(p.ws + WS_Q);
  gu64* const gP1L = (gu64*)(p.ws + WS_P1L);
  gu64* const gP1O = (gu64*)(p.ws + WS_P1O);
  gu64* const gP2 = (gu64*)(p.ws + WS_P2);
  gu64* const gP4 = (gu64*)(p.ws + WS_P4);

  const int grp = yc >> 3;
  const int mcol = xc * 64 + (yc & 7) * 8;
  const int m0 = grp == 0 ? 0 : (grp == 1 ? 2 : 3), m1 = grp == 0 ? 1 : (grp == 1 ? 5 : 4);
  u32x4 rA[4], rB[4];
  u32 c0 = 0, c1 = 0;
  if (grp < 3) {
    const char* wm0 = (const char*)(p.wmod + m0 * 512 + mcol);
    const char* wm1 = (const char*)(p.wmod + m1 * 512 + mcol);
    rA[0] = ldnt(wm0, u * 12288); rA[1] = ldnt(wm0 + 6144, u * 12288);
    rA[2] = ldnt(wm1, u * 12288); rA[3] = ldnt(wm1 + 6144, u * 12288);
    c0 = ((const u32*)p.c)[u]; c1 = ((const u32*)p.c)[256 + u];
  }
  const u32 epoch = __hip_atomic_load((gu32*)(p.ws + WS_EPOCH), RLX_AGENT);
  const u32x4 xa = *(const u32x4*)(p.x + lane * 8), xb = *(const u32x4*)(p.x + 512 + lane * 8);
  const int qcol = hkv * 512 + ((sp & 7) * 4 + bb * 2 + (sp >> 3)) * 16;
  {
    const char* wq = (const char*)(p.wq + qcol);
    rB[0] = ldnt(wq, u * 16384); rB[1] = ldnt(wq + 8192, u * 16384);
    rB[2] = ldnt(wq + 16, u * 16384); rB[3] = ldnt(wq + 8192 + 16, u * 16384);
  }
  if (u < 16) lds[L_CNT + u] = 0;
  SB();
  wg_barrier();
  const u32 tag = __builtin_amdgcn_readfirstlane(epoch) * 1664525u + 1013904223u;

  if (grp < 3) {
    auto silu = [](float z) { return z / (1.f + __expf(-z)); };
    const u32 s0 = pk_bf(silu(bf_lo(c0)), silu(bf_hi(c0))), s1 = pk_bf(silu(bf_lo(c1)), silu(bf_hi(c1)));
    u32 w[16];
    narrow_pack(rA, w);
    ldsf[L_RED + sw * 64 + lane] = narrow_dot(w, s0, s1, lane);
    lds_arrive(&lds[L_CNT + 0], lane);
    if (sw == 0) {
      float nrm = 0.f;
      if (grp == 0) {
        float sa_ = 0.f, sb_ = 0.f;
#pragma unroll
        for (int i = 0; i < 4; ++i) { sa_ += bf_lo(xa[i]) + bf_hi(xa[i]); sb_ += bf_lo(xb[i]) + bf_hi(xb[i]); }
        const float ma = wave_sum(sa_) * (1.f / 512), mb = wave_sum(sb_) * (1.f / 512);
        float va = 0.f, vb_ = 0.f;
#pragma unroll
        for (int i = 0; i < 4; ++i) {
          float d0 = bf_lo(xa[i]) - ma, d1 = bf_hi(xa[i]) - ma; va += d0 * d0 + d1 * d1;
          d0 = bf_lo(xb[i]) - mb; d1 = bf_hi(xb[i]) - mb; vb_ += d0 * d0 + d1 * d1;
        }
        va = wave_sum(va) * (1.f / 512); vb_ = wave_sum(vb_) * (1.f / 512);
        const int b = lane >> 5;
        const float xv = __uint_as_float((u32)p.x[b * 512 + mcol + ((lane >> 1) & 7)] << 16);
        const float mean = rbf(b ? mb : ma), var = rbf(b ? vb_ : va);
        nrm = rbf(rbf(xv - mean) / rbf(sqrtf(rbf(var + 1e-5f))));
      }
      lds_wait(&lds[L_CNT + 0], 4);
      const float r = rbf((ldsf[L_RED + lane] + ldsf[L_RED + 64 + lane]) + (ldsf[L_RED + 128 + lane] + ldsf[L_RED + 192 + lane]));
      const int b = lane >> 5, cp = (mcol >> 1) + ((lane >> 2) & 3);
      if (grp == 0) {
        auto s = __builtin_amdgcn_permlane16_swap(__float_as_uint(r), __float_as_uint(r), false, false);
        const float hv = rbf(rbf(nrm * rbf(1.f + __uint_as_float(s[1]))) + __uint_as_float(s[0]));
        const u32 pkd = pk_bf(hv, dpp<XOR2>(hv));
        if ((lane & 19) == 0) put(gH + cp * 2 + b, tag, pkd);
      } else {
        const u32 pkd = pk_bf(r, dpp<XOR2>(r));
        if ((lane & 3) == 0) put(gMX + ((grp - 1) * 2 + ((lane >> 4) & 1)) * 512 + cp * 2 + b, tag, pkd);
      }
    }
  }
  STAMP(sw == 0 ? 8 : 63);
  {
    u32 w[16];
    narrow_pack(rB, w);
    u32 hh[2];
    gather<2>([&](int i) { return gH + u * 2 + i; }, tag, hh);
    STAMP(sw == 0 ? 9 : 63);
    ldsf[L_RED2 + sw * 64 + lane] = narrow_dot(w, hh[0], hh[1], lane);
    lds_arrive(&lds[L_CNT + 1], lane);
    if (sw == 0) {
      lds_wait(&lds[L_CNT + 1], 4);
      const float r = (ldsf[L_RED2 + lane] + ldsf[L_RED2 + 64 + lane]) + (ldsf[L_RED2 + 128 + lane] + ldsf[L_RED2 + 192 + lane]);
      const u32 pkd = pk_bf(r, dpp<XOR2>(r));
      if ((lane & 3) == 0) put(gQ + (lane >> 5) * 2048 + (qcol >> 1) + ((lane >> 2) & 7), tag, pkd);
      STAMP(10);
      u32 qf[4];
      const int qi = bb * 2048 + hkv * 256 + ((lane & 15) >> 2) * 64 + ((lane >> 4) * 4 + (lane & 3)) * 4;
      gather<4>([&](int i) { return gQ + qi + i; }, tag, qf);
      *(u32x4*)&lds[L_Q + lane * 4] = u32x4{qf[0], qf[1], qf[2], qf[3]};
      lds_arrive(&lds[L_CNT + 2], lane);
      STAMP(11);
    }
  }
  const int jrow = bb * 16 + sp;
  const bool mlp = yc < 28;
  const int fcol = (xc * 28 + (mlp ? yc : 0)) * 8;
  u32 wo[16], wgu[16], wd[8], gab[4], scb[4];
  {
    u32 t[16];
    const char* b0 = (const char*)(p.wo + (size_t)(hkv * 512 + jrow * 16) * 512);
#pragma unroll
    for (int i = 0; i < 16; ++i) t[i] = ldnt1(b0 + i * 1024, u * 4);
    u32x4 r[4];
    const char* g0 = (const char*)(p.wg + fcol); const char* u0 = (const char*)(p.wu + fcol);
    r[0] = ldnt(g0, u * 7168); r[1] = ldnt(g0 + 3584, u * 7168);
    r[2] = ldnt(u0, u * 7168); r[3] = ldnt(u0 + 3584, u * 7168);
    u32 t2[8];
    const char* d0 = (const char*)(p.wd + (size_t)fcol * 512);
#pragma unroll
    for (int i = 0; i < 8; ++i) t2[i] = ldnt1(d0 + i * 1024, u * 4);
#pragma unroll
    for (int i = 0; i < 8; ++i) { wo[2 * i] = perm_lo(t[2 * i], t[2 * i + 1]); wo[2 * i + 1] = perm_hi(t[2 * i], t[2 * i + 1]); }
    narrow_pack(r, wgu);
#pragma unroll
    for (int i = 0; i < 4; ++i) { wd[2 * i] = perm_lo(t2[2 * i], t2[2 * i + 1]); wd[2 * i + 1] = perm_hi(t2[2 * i], t2[2 * i + 1]); }
    gather<4>([&](int i) { return gMX + (i >> 1) * 512 + tcol * 2 + (i & 1); }, tag, gab);
    gather<4>([&](int i) { return gMX + (2 + (i >> 1)) * 512 + u * 2 + (i & 1); }, tag, scb);
  }
  const u32 xres0 = ((const u32*)p.x)[tcol], xres1 = ((const u32*)p.x)[256 + tcol];
#pragma unroll
  for (int i = 0; i < 16; ++i) asm volatile("" : "+v"(wo[i]), "+v"(wgu[i]));
#pragma unroll
  for (int i = 0; i < 8; ++i) asm volatile("" : "+v"(wd[i]));
  asm volatile("" ::"v"(xres0), "v"(xres1));
  STAMP(sw == 0 ? 12 : 63);
  wg_barrier();
  STAMP(sw == 0 ? 13 : 63);

  {
    float mw[4], lw[4];
#pragma unroll
    for (int w = 0; w < 4; ++w) { mw[w] = ldsf[L_ML + w * 8 + sw]; lw[w] = ldsf[L_ML + w * 8 + 4 + sw]; }
    const float M = fmaxf(fmaxf(mw[0], mw[1]), fmaxf(mw[2], mw[3]));
    float L = 0.f, o0 = 0.f, o1 = 0.f;
#pragma unroll
    for (int w = 0; w < 4; ++w) {
      const float f = __builtin_amdgcn_exp2f((mw[w] - M) * SCL);
      const f32x2 ov = *(const f32x2*)&ldsf[L_O + w * 512 + sw * 128 + 2 * lane];
      L = fmaf(f, lw[w], L); o0 = fmaf(f, ov[0], o0); o1 = fmaf(f, ov[1], o1);
    }
    const float inv = 1.f / L;
    putf(gP1O + wg * 512 + u * 2, tag, o0 * inv);
    putf(gP1O + wg * 512 + u * 2 + 1, tag, o1 * inv);
    if (lane == 0) putf(gP1L + wg * 16 + sw, tag, fmaf(M, SCL, __builtin_amdgcn_logf(L)));
  }
  STAMP(sw == 0 ? 14 : 63);
  {
    const int pi = u >> 3, wp = ((pi >> 4) * 8 + hkv) * 16 + (pi & 15);
    u32 g[3];
    gather<3>([&](int i) { return i < 2 ? gP1O + wp * 512 + jrow * 16 + (u & 7) * 2 + i : gP1L + wp * 16 + (jrow >> 3); }, tag, g);
    STAMP(sw == 0 ? 15 : 63);
    const float lse = __uint_as_float(g[2]);
    float M = fmaxf(lse, dpp<ROR8>(lse));
    M = max32(max16(M));
    float wt = __builtin_amdgcn_exp2f(lse - M);
    float v0 = wt * __uint_as_float(g[0]), v1 = wt * __uint_as_float(g[1]);
    wt += dpp<ROR8>(wt); v0 += dpp<ROR8>(v0); v1 += dpp<ROR8>(v1);
    const float r = fold32(fold16(v0, v1), fold16(wt, wt));
    ldsf[L_RED3 + sw * 64 + lane] = lane < 48 ? r : M;
    wg_barrier();
    const int b = (lane >> 3) & 1, pr = lane & 7;
    const float* ra = ldsf + L_RED3 + b * 128; const float* rb = ra + 64;
    const float Ma = ra[48], Mb = rb[48], Mx = fmaxf(Ma, Mb);
    const float fa = __builtin_amdgcn_exp2f(Ma - Mx), fb = __builtin_amdgcn_exp2f(Mb - Mx);
    const float inv = 1.f / (fa * ra[32] + fb * rb[32]);
    const u32 ob = pk_bf((fa * ra[pr] + fb * rb[pr]) * inv, (fa * ra[16 + pr] + fb * rb[16 + pr]) * inv);
    float y[4] = {0.f, 0.f, 0.f, 0.f};
#pragma unroll
    for (int i = 0; i < 8; ++i) {
      const u32 a0 = __builtin_amdgcn_readlane(ob, i), a1 = __builtin_amdgcn_readlane(ob, 8 + i);
      y[0] = dot2(wo[2 * i], a0, y[0]); y[1] = dot2(wo[2 * i + 1], a0, y[1]);
      y[2] = dot2(wo[2 * i], a1, y[2]); y[3] = dot2(wo[2 * i + 1], a1, y[3]);
    }
#pragma unroll
    for (int i = 0; i < 4; ++i) putf(gP2 + wg * 1024 + u * 4 + i, tag, y[i]);
  }
  STAMP(sw == 0 ? 16 : 63);
  float x2own;
  {
    u32 g[4];
    gather<4>([&](int i) { return gP2 + u * 1024 + tcol * 4 + i; }, tag, g);
    STAMP(sw == 0 ? 17 : 63);
    const float r = wave_sum4(__uint_as_float(g[0]), __uint_as_float(g[1]), __uint_as_float(g[2]), __uint_as_float(g[3]));
    ldsf[L_RED3 + 256 + sw * 64 + lane] = r;
    wg_barrier();
    const float* rr = ldsf + L_RED3 + 256 + lane;
    const float att = rbf((rr[0] + rr[64]) + (rr[128] + rr[192]));
    const u32 gw = lane < 32 ? gab[0] : gab[1], xw = lane < 32 ? xres0 : xres1;
    const float ga = (lane & 16) ? bf_hi(gw) : bf_lo(gw), xv = (lane & 16) ? bf_hi(xw) : bf_lo(xw);
    x2own = rbf(xv + rbf(ga * att));
    auto s = __builtin_amdgcn_permlane16_swap(__float_as_uint(x2own), __float_as_uint(x2own), false, false);
    if (sw == 0 && (lane & 31) == 0) put(gX2 + tcol * 2 + (lane >> 5), tag, pk_bf(__uint_as_float(s[0]), __uint_as_float(s[1])));
    if (wg == 0 && u == 0) __hip_atomic_store((gu32*)(p.ws + WS_EPOCH), tag, RLX_AGENT);
  }
  STAMP(sw == 0 ? 18 : 63);
  if (mlp) {
    u32 g[8];
    gather<8>([&](int i) { return gX2 + (((sw + (i >> 1)) & 3) * 64 + lane) * 2 + (i & 1); }, tag, g);
    STAMP(sw == 0 ? 19 : 63);
    float s0 = 0.f, s1 = 0.f;
#pragma unroll
    for (int i = 0; i < 4; ++i) { s0 += bf_lo(g[2 * i]) + bf_hi(g[2 * i]); s1 += bf_lo(g[2 * i + 1]) + bf_hi(g[2 * i + 1]); }
    const float mu0 = wave_sum(s0) * (1.f / 512), mu1 = wave_sum(s1) * (1.f / 512);
    float q0 = 0.f, q1 = 0.f;
#pragma unroll
    for (int i = 0; i < 4; ++i) {
      float d0 = bf_lo(g[2 * i]) - mu0, d1 = bf_hi(g[2 * i]) - mu0; q0 += d0 * d0 + d1 * d1;
      d0 = bf_lo(g[2 * i + 1]) - mu1; d1 = bf_hi(g[2 * i + 1]) - mu1; q1 += d0 * d0 + d1 * d1;
    }
    const float sd0 = rbf(sqrtf(rbf(rbf(wave_sum(q0) * (1.f / 512)) + 1e-5f))), sd1 = rbf(sqrtf(rbf(rbf(wave_sum(q1) * (1.f / 512)) + 1e-5f)));
    const float mr0 = rbf(mu0), mr1 = rbf(mu1);
    auto mod = [](float xv, float mean, float sd, float cb, float sb) { return rbf(rbf(rbf(xv - mean) / sd) * rbf(1.f + cb)) + sb; };
    const u32 h0 = pk_bf(mod(bf_lo(g[0]), mr0, sd0, bf_lo(scb[2]), bf_lo(scb[0])), mod(bf_hi(g[0]), mr0, sd0, bf_hi(scb[2]), bf_hi(scb[0])));
    const u32 h1 = pk_bf(mod(bf_lo(g[1]), mr1, sd1, bf_lo(scb[3]), bf_lo(scb[1])), mod(bf_hi(g[1]), mr1, sd1, bf_hi(scb[3]), bf_hi(scb[1])));
    ldsf[L_RED3 + 512 + sw * 64 + lane] = narrow_dot(wgu, h0, h1, lane);
    wg_barrier();
    const int gi = ((lane >> 3) & 1) * 32 + (lane & 7) * 2;
    const float* rr = ldsf + L_RED3 + 512 + gi;
    const float gv = rbf((rr[0] + rr[64]) + (rr[128] + rr[192])), uv = rbf((rr[16] + rr[80]) + (rr[144] + rr[208]));
    const float inner = 0.7978845608028654f * (gv + 0.044715f * gv * gv * gv);
    const float th = 1.f - 2.f / (1.f + __expf(2.f * inner));
    const float h3 = rbf(0.5f * gv * (1.f + th)) * uv;
    const u32 hb = pk_bf(h3, dpp<XOR1>(h3));
    float y[4] = {0.f, 0.f, 0.f, 0.f};
#pragma unroll
    for (int i = 0; i < 4; ++i) {
      const u32 a0 = __builtin_amdgcn_readlane(hb, 2 * i), a1 = __builtin_amdgcn_readlane(hb, 8 + 2 * i);
      y[0] = dot2(wd[2 * i], a0, y[0]); y[1] = dot2(wd[2 * i + 1], a0, y[1]);
      y[2] = dot2(wd[2 * i], a1, y[2]); y[3] = dot2(wd[2 * i + 1], a1, y[3]);
    }
    const int slot = xc * 28 + yc;
#pragma unroll
    for (int i = 0; i < 4; ++i) putf(gP4 + slot * 1024 + u * 4 + i, tag, y[i]);
  }
  STAMP(sw == 0 ? 20 : 63);
  {
    u32 g[4];
    const bool act = u < 224;
    const int us = act ? u : 0;
    gather<4>([&](int i) { return gP4 + us * 1024 + tcol * 4 + i; }, tag, g);
    STAMP(sw == 0 ? 21 : 63);
    const float z = act ? 1.f : 0.f;
    const float r = wave_sum4(z * __uint_as_float(g[0]), z * __uint_as_float(g[1]), z * __uint_as_float(g[2]), z * __uint_as_float(g[3]));
    ldsf[L_RED3 + 768 + sw * 64 + lane] = r;
    wg_barrier();
    if (sw == 0) {
      const float* rr = ldsf + L_RED3 + 768 + lane;
      const float dn = rbf((rr[0] + rr[64]) + (rr[128] + rr[192]));
      const u32 gw = lane < 32 ? gab[2] : gab[3];
      const float gb = (lane & 16) ? bf_hi(gw) : bf_lo(gw);
      const float res = x2own + rbf(gb * dn);
      auto s = __builtin_amdgcn_permlane16_swap(__float_as_uint(res), __float_as_uint(res), false, false);
      if ((lane & 31) == 0) ((u32*)p.out)[(lane >> 5) * 256 + tcol] = pk_bf(__uint_as_float(s[0]), __uint_as_float(s[1]));
    }
  }
  STAMP(sw == 0 ? 22 : 63);
}

extern "C" void kernel_launch(void* const* d_in, const int* in_sizes, int n_in, void* d_out, int out_size, void* d_ws,
                              size_t ws_size, hipStream_t stream) {
  Params p;
  p.x = (const u16*)d_in[0];
  p.k = (const u16*)d_in[1];
  p.v = (const u16*)d_in[2];
  p.wq = (const u16*)d_in[3];
  p.wo = (const u16*)d_in[4];
  p.wg = (const u16*)d_in[6];
  p.wu = (const u16*)d_in[7];
  p.wd = (const u16*)d_in[8];
  p.c = (const u16*)d_in[9];
  p.wmod = (const u16*)d_in[10];
  p.out = (u16*)d_out;
  p.ws = (char*)d_ws;
  fused_block_kernel<<<NWG, NTHR, 0, stream>>>(p);
}
```

```cpp
#include <hip/hip_runtime.h>
#include <hip/hip_bf16.h>

typedef unsigned u32;
typedef unsigned short u16;
typedef unsigned long long u64;
typedef u32 u32x4 __attribute__((ext_vector_type(4)));
typedef u32 u32x2 __attribute__((ext_vector_type(2)));
typedef float f32x4 __attribute__((ext_vector_type(4)));
typedef float f32x2 __attribute__((ext_vector_type(2)));
typedef short s16x4 __attribute__((ext_vector_type(4)));
typedef __bf16 bf16x8 __attribute__((ext_vector_type(8)));
typedef __bf16 bf16x2 __attribute__((ext_vector_type(2)));
typedef __attribute__((address_space(1))) u64 gu64;
typedef __attribute__((address_space(1))) u32 gu32;
typedef __attribute__((address_space(3))) u32 lu32;

#define RLX_AGENT __ATOMIC_RELAXED, __HIP_MEMORY_SCOPE_AGENT
#define DEVI __device__ __forceinline__

#define NPRE 5
#define NL 4

constexpr int D = 512, SK = 32768, DF = 1792;
constexpr int NWG = 256, NTHR = 512;
constexpr int TILES = 32;
constexpr float SCL = 0.08838834764831845f * 1.4426950408889634f;
constexpr u32 SPIN_CAP = 1u << 18;

constexpr u32 WS_EPOCH = 0;
constexpr u32 WS_CLK = 512;
constexpr u32 WS_MX  = 4096;
constexpr u32 WS_Q   = WS_MX + 192 * 128;
constexpr u32 WS_CLR = WS_Q + 256 * 128;
constexpr u32 WS_GO  = WS_CLR + 4096;
constexpr u32 WS_P2  = WS_GO + 64 * 128;
constexpr u32 WS_P1  = WS_P2 + 256 * 128;
constexpr u32 WS_P4  = WS_P1 + 1024 * 640;
constexpr u32 WS_END = WS_P4 + 56 * 4096;
constexpr u32 EMPTY = 0xffffffffu;

struct Params {
  const u16 *x, *k, *v, *wq, *wo, *wg, *wu, *wd, *c, *wmod;
  u16* out;
  char* ws;
};

DEVI float bf_lo(u32 w) { return __uint_as_float(w << 16); }
DEVI float bf_hi(u32 w) { return __uint_as_float(w & 0xffff0000u); }
DEVI u32 pk_bf(float lo, float hi) {
  bf16x2 r = {(__bf16)lo, (__bf16)hi};
  return __builtin_bit_cast(u32, r);
}
DEVI float rbf(float x) { return (float)(__bf16)x; }
DEVI f32x2 up2(u32 w) { return f32x2{bf_lo(w), bf_hi(w)}; }
DEVI f32x2 rbf2(f32x2 x) { return up2(pk_bf(x[0], x[1])); }
DEVI float dot2(u32 a, u32 b, float c) {
  return __builtin_amdgcn_fdot2_f32_bf16(__builtin_bit_cast(bf16x2, a), __builtin_bit_cast(bf16x2, b), c, false);
}
DEVI u32 perm_lo(u32 a, u32 b) { return __builtin_amdgcn_perm(b, a, 0x05040100u); }
DEVI u32 perm_hi(u32 a, u32 b) { return __builtin_amdgcn_perm(b, a, 0x07060302u); }
template <int CTRL> DEVI float dpp(float x) {
  return __builtin_bit_cast(float, __builtin_amdgcn_mov_dpp(__builtin_bit_cast(int, x), CTRL, 0xf, 0xf, true));
}
constexpr int XOR1 = 0xB1, XOR2 = 0x4E, XOR7 = 0x141, ROR8 = 0x128;
DEVI float fold32(float a, float b) {
  auto s = __builtin_amdgcn_permlane32_swap(__float_as_uint(a), __float_as_uint(b), false, false);
  return __uint_as_float(s[0]) + __uint_as_float(s[1]);
}
DEVI float fold16(float a, float b) {
  auto s = __builtin_amdgcn_permlane16_swap(__float_as_uint(a), __float_as_uint(b), false, false);
  return __uint_as_float(s[0]) + __uint_as_float(s[1]);
}
DEVI float max32(float a) {
  auto s = __builtin_amdgcn_permlane32_swap(__float_as_uint(a), __float_as_uint(a), false, false);
  return fmaxf(__uint_as_float(s[0]), __uint_as_float(s[1]));
}
DEVI float max16(float a) {
  auto s = __builtin_amdgcn_permlane16_swap(__float_as_uint(a), __float_as_uint(a), false, false);
  return fmaxf(__uint_as_float(s[0]), __uint_as_float(s[1]));
}
DEVI float row_sum(float x) { x += dpp<XOR1>(x); x += dpp<XOR2>(x); x += dpp<XOR7>(x); x += dpp<ROR8>(x); return x; }
DEVI float wave_sum(float x) { x = row_sum(x); x = fold16(x, x); return fold32(x, x); }
DEVI u32x4 ldnt(const void* base, u32 off) { return __builtin_nontemporal_load((const u32x4*)((const char*)base + off)); }
DEVI u32 ldnt1(const void* base, u32 off) { return __builtin_nontemporal_load((const u32*)((const char*)base + off)); }
template <int NT> DEVI u32x4 ldp(const void* base, u32 off) { return NT ? ldnt(base, off) : *(const u32x4*)((const char*)base + off); }
#define PRNT 1
#define NRNT 1
typedef __amdgpu_buffer_rsrc_t rsrc_t;
constexpr int SC1 = 16;
DEVI u32x4 ld16(rsrc_t r, u32 off, u32 so = 0) { return __builtin_amdgcn_raw_buffer_load_b128(r, off, so, SC1); }
DEVI u32x2 ld8(rsrc_t r, u32 off) { return __builtin_amdgcn_raw_buffer_load_b64(r, off, 0, SC1); }
DEVI u32 ld4(rsrc_t r, u32 off) { return __builtin_amdgcn_raw_buffer_load_b32(r, off, 0, SC1); }
DEVI void st16(rsrc_t r, u32 off, u32x4 v) { __builtin_amdgcn_raw_buffer_store_b128(v, r, off, 0, SC1); }
DEVI void st8(rsrc_t r, u32 off, u32x2 v) { __builtin_amdgcn_raw_buffer_store_b64(v, r, off, 0, SC1); }
DEVI void st4(rsrc_t r, u32 off, u32 v) { __builtin_amdgcn_raw_buffer_store_b32(v, r, off, 0, SC1); }
DEVI u32 full(u32 v) { return v == EMPTY ? 0x7fff7fffu : v; }
DEVI u32 full(float v) { return full(__float_as_uint(v)); }
DEVI void drain() { asm volatile("s_waitcnt vmcnt(0)" ::: "memory"); }
DEVI bool none(bool x) { return __builtin_amdgcn_ballot_w64(x) == 0; }
DEVI bool has_empty(u32x4 v) { return max(max(v[0], v[1]), max(v[2], v[3])) == EMPTY; }
template <int N, int SLP = 2, typename F> DEVI void gather_tag(rsrc_t r, F off, u32 tag, u32 (&v)[2 * N]) {
#pragma unroll 1
  for (u32 spins = 0; spins < SPIN_CAP; ++spins) {
    asm volatile("" ::: "memory");
    bool bad = false;
    u32x4 x[N];
#pragma unroll
    for (int i = 0; i < N; ++i) x[i] = ld16(r, off(i));
#pragma unroll
    for (int i = 0; i < N; ++i) { v[2 * i] = x[i][0]; v[2 * i + 1] = x[i][2]; bad |= x[i][1] != tag || x[i][3] != tag; }
    if (none(bad)) break;
    __builtin_amdgcn_s_sleep(SLP);
  }
}
template <int N, u32 STEP> DEVI f32x4 gather_sum(rsrc_t r, u32 off) {
  u32x4 v[N];
#pragma unroll
  for (int i = 0; i < N; ++i) asm volatile("" : "=v"(v[i]));
#pragma unroll 1
  for (u32 spins = 0; spins < SPIN_CAP; ++spins) {
    asm volatile("" ::: "memory");
    u32 m = 0;
#pragma unroll
    for (int i = 0; i < N; ++i) v[i] = ld16(r, off, i * STEP);
#pragma unroll
    for (int i = 0; i < N; ++i) m = max(max(m, max(v[i][0], v[i][1])), max(v[i][2], v[i][3]));
    if (none(m == EMPTY)) break;
  }
  __builtin_amdgcn_s_waitcnt(0x0f70);
#pragma unroll
  for (int w = 1; w < N; w *= 2)
#pragma unroll
    for (int i = 0; i + w < N; i += 2 * w) v[i] = __builtin_bit_cast(u32x4, __builtin_bit_cast(f32x4, v[i]) + __builtin_bit_cast(f32x4, v[i + w]));
  return __builtin_bit_cast(f32x4, v[0]);
}
DEVI void lds_wait_lgkm() { asm volatile("s_waitcnt lgkmcnt(0)" ::: "memory"); }
DEVI void wg_barrier() { asm volatile("s_waitcnt lgkmcnt(0)\n\ts_barrier" ::: "memory"); }
DEVI void lds_arrive(u32* cnt, int lane) {
  lds_wait_lgkm();
  if (lane == 0) __hip_atomic_fetch_add(cnt, 1u, __ATOMIC_RELAXED, __HIP_MEMORY_SCOPE_WORKGROUP);
}
DEVI void lds_wait(u32* cnt, u32 want) {
#pragma unroll 1
  for (u32 spins = 0; spins < (SPIN_CAP << 6); ++spins) {
    if (__builtin_amdgcn_readfirstlane(__hip_atomic_load(cnt, __ATOMIC_RELAXED, __HIP_MEMORY_SCOPE_WORKGROUP)) >= want) break;
    __builtin_amdgcn_s_sleep(1);
  }
  asm volatile("" ::: "memory");
}

DEVI float wave_sum4(float v0, float v1, float v2, float v3) { return row_sum(fold16(fold32(v0, v2), fold32(v1, v3))); }

struct Att { f32x4 acc[8]; float m, l; bf16x8 q; };

#define KPOL 2
#define VPOL 2
template <int AUX> DEVI void ld_tile(u32x4 (&d)[4], rsrc_t r, u32 off) {
#pragma unroll
  for (int i = 0; i < 4; ++i) d[i] = __builtin_amdgcn_raw_buffer_load_b128(r, off + i * 1024, 0, AUX);
}
DEVI void park(u32x4* s, const u32x4 (&d)[4]) {
#pragma unroll
  for (int i = 0; i < 4; ++i) s[i * 64] = d[i];
}
DEVI void unpark(u32x4 (&d)[4], const u32x4* s) {
#pragma unroll
  for (int i = 0; i < 4; ++i) d[i] = s[i * 64];
}
DEVI void qk(const Att& a, const u32x4 (&k)[4], f32x4 (&s)[4]) {
#pragma unroll
  for (int i = 0; i < 4; ++i)
    s[i] = __builtin_amdgcn_mfma_f32_16x16x32_bf16(__builtin_bit_cast(bf16x8, k[i]), a.q, f32x4{0.f, 0.f, 0.f, 0.f}, 0, 0, 0);
}
DEVI s16x4 softmax_step(Att& a, const f32x4 (&d)[4]) {
  float s[4];
#pragma unroll
  for (int i = 0; i < 4; ++i) s[i] = (dpp<0x00>(d[i][0]) + dpp<0x55>(d[i][1])) + (dpp<0xAA>(d[i][2]) + dpp<0xFF>(d[i][3]));
  float t = fmaxf(fmaxf(s[0], s[1]), fmaxf(s[2], s[3]));
  t = max32(max16(t));
  if (__builtin_amdgcn_ballot_w64(t > a.m) != 0) {
    const float mn = fmaxf(a.m, t);
    const float al = __builtin_amdgcn_exp2f((a.m - mn) * SCL);
    a.m = mn; a.l *= al;
#pragma unroll
    for (int j = 0; j < 8; ++j) a.acc[j] *= al;
  }
  const float mc = a.m * SCL;
  float pr[4];
#pragma unroll
  for (int i = 0; i < 4; ++i) pr[i] = __builtin_amdgcn_exp2f(fmaf(s[i], SCL, -mc));
  a.l += (pr[0] + pr[1]) + (pr[2] + pr[3]);
  u32x2 pk = {pk_bf(pr[0], pr[1]), pk_bf(pr[2], pr[3])};
  return __builtin_bit_cast(s16x4, pk);
}
DEVI void vtrans(const u32x4 (&v)[4], u32x2 (&t)[8]) {
#pragma unroll
  for (int i = 0; i < 4; ++i) {
    t[2 * i]     = u32x2{perm_lo(v[0][i], v[1][i]), perm_lo(v[2][i], v[3][i])};
    t[2 * i + 1] = u32x2{perm_hi(v[0][i], v[1][i]), perm_hi(v[2][i], v[3][i])};
  }
}
DEVI void pv(Att& a, const u32x2 (&t)[8], s16x4 p) {
#pragma unroll
  for (int j = 0; j < 8; ++j) a.acc[j] = __builtin_amdgcn_mfma_f32_16x16x16bf16_1k(__builtin_bit_cast(s16x4, t[j]), p, a.acc[j], 0, 0, 0);
}
#define SB() __builtin_amdgcn_sched_barrier(0)
#define OFF 100
#define PST 100
#define OFQ 250
#define SPAN 2850
#define DO 100
DEVI u32 now() { return (u32)__builtin_amdgcn_s_memrealtime() << 4; }
DEVI void hold(u32 due) {
#pragma unroll 1
  for (int i = 0; i < 64 && (int)(now() - due) < 0; ++i) __builtin_amdgcn_s_sleep(2);
}
template <bool REFILL> DEVI void tile_step(Att& a, u32x4 (&k)[4], u32x4 (&v)[4], rsrc_t kn, rsrc_t vn, u32 offk, u32 offv, u32 due) {
  f32x4 s[4]; u32x2 t[8];
  qk(a, k, s);
  SB();
  if (REFILL) hold(due);
  if (REFILL) ld_tile<KPOL>(k, kn, offk);
  SB();
  const s16x4 p = softmax_step(a, s);
  SB();
  vtrans(v, t);
  SB();
  if (REFILL) ld_tile<VPOL>(v, vn, offv);
  SB();
  pv(a, t, p);
  SB();
}
DEVI void touch(const u32x4 (&d)[4]) { asm volatile("" ::"v"(d[0]), "v"(d[1]), "v"(d[2]), "v"(d[3])); }

constexpr int L_CNT = 0;
constexpr int L_ZERO = 16;
constexpr int L_Q = 32;
constexpr int L_ACT = L_Q + 256;
constexpr int L_ACT2 = L_ACT + 520;
constexpr int L_RED = L_ACT2 + 128;
constexpr int L_STAT = L_RED + 1024;
constexpr int L_ML = L_STAT + 16;
constexpr int L_O = L_ML + 32;
constexpr int L_T = L_O + 2048;
constexpr int L_END = L_T + 4 * NL * 2048;

#define PACE 4
template <int NT, bool PC> DEVI void tile_load(u32x4 (&w)[16], const char* base, u32 stride) {
#pragma unroll
  for (int i = 0; i < 16; ++i) {
    w[i] = ldp<NT>(base, i * stride);
    if (PC && i % PACE == PACE - 1) { asm volatile("" ::"v"(w[i])); SB(); }
  }
}
DEVI void pack8(u32x4* f, const u32x4* r) {
  u32x4 o[8];
#pragma unroll
  for (int j = 0; j < 4; ++j)
#pragma unroll
    for (int t = 0; t < 4; ++t) { o[2 * j][t] = perm_lo(r[2 * t][j], r[2 * t + 1][j]); o[2 * j + 1][t] = perm_hi(r[2 * t][j], r[2 * t + 1][j]); }
#pragma unroll
  for (int j = 0; j < 8; ++j) { f[j] = o[j]; asm volatile("" : "+v"(f[j])); }
}
DEVI void tile_pack(u32x4 (&w)[16]) { pack8(w, w); pack8(w + 8, w + 8); }
DEVI void tile_mul(const u32x4 (&w)[16], u32* lds, const u32 (&ao)[4], u32 wr, int lane) {
  asm volatile("" ::: "memory");
  bf16x8 a[8];
#pragma unroll
  for (int i = 0; i < 8; ++i) a[i] = __builtin_bit_cast(bf16x8, *(const u32x4*)&lds[ao[i >> 1] + (i & 1) * 4]);
  SB();
  f32x4 e = {0.f, 0.f, 0.f, 0.f}, o = e;
#pragma unroll
  for (int i = 0; i < 8; ++i) {
    e = __builtin_amdgcn_mfma_f32_16x16x32_bf16(a[i], __builtin_bit_cast(bf16x8, w[(i & 1) * 8 + (i & 6)]), e, 0, 0, 0);
    o = __builtin_amdgcn_mfma_f32_16x16x32_bf16(a[i], __builtin_bit_cast(bf16x8, w[(i & 1) * 8 + (i & 6) + 1]), o, 0, 0, 0);
  }
  *(f32x4*)&lds[wr] = f32x4{e[0] + dpp<ROR8>(e[2]), o[0] + dpp<ROR8>(o[2]), e[1] + dpp<ROR8>(e[3]), o[1] + dpp<ROR8>(o[3])};
}
DEVI f32x2 tile_red(const float* ldsf, u32 rd) {
  const f32x2* r = (const f32x2*)&ldsf[rd];
  return (r[0] + r[64]) + (r[128] + r[192]);
}
DEVI u32 pin(u32 x) { asm volatile("" : "+v"(x)); return x; }

#define SLPW 127
#define QSL 8
#define DIRECT 3
#define SLP2 1
#define TB0 0x5771111444455555ull
#define TB1 0x4575111454455454ull
constexpr int nibsum(u64 t) { int s = 0; for (int i = 0; i < 16; ++i) s += t >> 4 * i & 15; return s; }
static_assert(nibsum(TB0) == 64 && nibsum(TB1) == 64, "the shares must cover the cache");
#define EARLY 10
#define EWT 4
#define SLP0 2
#define SLPH 8
#define SLPQ 8
#define SLP1 1
#define SLP3 40

__global__ void __launch_bounds__(NTHR, 2) fused_block_kernel(Params p) {
  __shared__ __attribute__((aligned(16))) u32 lds[L_END];
  float* ldsf = (float*)lds;
  const int tid = threadIdx.x, lane = tid & 63;
  const int wave = __builtin_amdgcn_readfirstlane(tid >> 6);
  const int wg = blockIdx.x;
  const int xc = wg & 7, yc = wg >> 3;
  const int pair = (yc + 2 * xc) & 15, bb = pair >> 3, hkv = pair & 7, rec = pair * 16 + (yc >> 4) * 8 + xc;
  const rsrc_t ws = __builtin_amdgcn_make_buffer_rsrc(p.ws, 0, WS_END, 0x00020000);

  if (wave < 4) {
    const u64 tb = (yc & 1) ? TB1 : TB0, lo = tb & ((1ull << (yc >> 1) * 4) - 1);
    const int ntile = 28 + (int)(tb >> (yc >> 1) * 4 & 15);
    const int first = 28 * (yc >> 1) + (int)(((lo & 0x0f0f0f0f0f0f0f0full) + (lo >> 4 & 0x0f0f0f0f0f0f0f0full)) * 0x0101010101010101ull >> 56);
    const size_t kvo = ((size_t)pair * 512 + first) * 16384;
    const rsrc_t rk = __builtin_amdgcn_make_buffer_rsrc((char*)p.k + kvo, 0, ntile * 16384, 0x00020000);
    const rsrc_t rv = __builtin_amdgcn_make_buffer_rsrc((char*)p.v + kvo, 0, ntile * 16384, 0x00020000);
    u32 ok = wave * 4096 + ((lane & 15) >> 2) * 256 + (lane >> 4) * 64 + (lane & 3) * 16, ov = wave * 4096 + lane * 16;
    u32x4* const slots = (u32x4*)&lds[L_T + wave * NL * 2048] + lane;
    u32x4 K[NPRE][4], V[NPRE][4];
    u32 step = PST << 4, due = now() + (OFF << 4) - step;
    const u32 tk = __builtin_amdgcn_raw_buffer_load_b32((wave & 1) ? rv : rk, (wave >> 1) * (ntile * 16384 - 4), 0, 2);
    SB();
    if (yc >= EARLY) {
      ld_tile<KPOL>(K[0], rk, ok); SB();
      ld_tile<VPOL>(V[0], rv, ov); SB();
      wg_barrier();
    } else {
      wg_barrier();
      __builtin_amdgcn_s_sleep(SLP0); SB();
      ld_tile<KPOL>(K[0], rk, ok); SB();
      ld_tile<VPOL>(V[0], rv, ov);
    }
    SB();
    asm volatile("" ::"v"(tk));
    touch(K[0]);
#pragma unroll 1
    for (int t = 0; t < NL; ++t) {
      ok += 16384; ov += 16384; due += step;
      park(slots + t * 512, K[0]); SB();
      hold(due);
      ld_tile<KPOL>(K[0], rk, ok); SB();
      park(slots + t * 512 + 256, V[0]); SB();
      ld_tile<VPOL>(V[0], rv, ov); SB();
    }
#pragma unroll
    for (int t = 1; t < NPRE; ++t) {
      ok += 16384; ov += 16384; due += step;
      touch(K[t - 1]); SB();
      hold(due);
      ld_tile<KPOL>(K[t], rk, ok); SB();
      touch(V[t - 1]); SB();
      ld_tile<VPOL>(V[t], rv, ov); SB();
      if (t == EWT) { lds_arrive(&lds[L_CNT + 8], lane); SB(); }
    }
    lds_wait(&lds[L_CNT + 2], 1);
    constexpr int NB = NL + NPRE - 1;
    step = ((SPAN - (yc >= 10 && yc < 18 ? DO : 0)) << 4) / (u32)(ntile - NB - 2);
    due = __builtin_amdgcn_readfirstlane(lds[L_CNT + 10]) + (OFQ << 4) - step;
    Att a;
    a.q = __builtin_bit_cast(bf16x8, *(const u32x4*)&lds[L_Q + lane * 4]);
    a.m = -INFINITY; a.l = 0.f;
#pragma unroll
    for (int j = 0; j < 8; ++j) a.acc[j] = f32x4{0.f, 0.f, 0.f, 0.f};
#pragma unroll 1
    for (int t = 0; t < ntile - NB; ++t) {
      ok += 16384; ov += 16384;
      if (t < ntile - NB - 1) due += step;
      tile_step<true>(a, K[0], V[0], rk, rv, ok, ov, due);
      if (t < NB) {
        tile_step<false>(a, K[1], V[1], rk, rv, 0, 0, 0);
        if (t >= NPRE - 2) {
          const u32x4* const s = slots + min(t - (NPRE - 2), NL - 1) * 512;
          unpark(K[1], s); unpark(V[1], s + 256);
        }
#pragma unroll
        for (int i = 2; i < NPRE; ++i)
          if (t == i - 2) {
#pragma unroll
            for (int j = 0; j < 4; ++j) { K[1][j] = K[i][j]; V[1][j] = V[i][j]; asm volatile("" : "+v"(K[1][j]), "+v"(V[1][j])); }
          }
        SB();
      }
    }
    {
      const float lsum = fold32(fold16(a.l, a.l), fold16(a.l, a.l));
      const int qq = lane & 3, h = (lane & 15) >> 2, c = lane >> 4;
      float o[8];
#pragma unroll
      for (int j = 0; j < 8; ++j) { const float lo = (qq & 1) ? a.acc[j][1] : a.acc[j][0], hi = (qq & 1) ? a.acc[j][3] : a.acc[j][2]; o[j] = (qq & 2) ? hi : lo; }
      float* rp = ldsf + L_O + wave * 512 + h * 128 + (4 * c + qq) * 8;
      *(f32x4*)rp = f32x4{o[0], o[1], o[2], o[3]};
      *(f32x4*)(rp + 4) = f32x4{o[4], o[5], o[6], o[7]};
      if (lane < 16 && qq == 0) { ldsf[L_ML + h * 8 + wave] = a.m; ldsf[L_ML + h * 8 + 4 + wave] = lsum; }
    }
    wg_barrier();
    touch(K[0]); touch(V[0]);
    return;
  }

  const int sw = wave - 4, u = tid - 256;
  const int hf = lane >> 5, pb = (lane >> 4) & 1;
  const bool rA = yc < 2, rQ = yc >= 2 && yc < 10, rO = yc >= 10 && yc < 18, rM = yc >= 18 && yc < 25, rF = yc == 25 || yc > 28;
  const int fi = xc * 4 + (yc == 25 ? 0 : yc - 28), mi = xc * 7 + yc - 18;
  const int tile = rA ? xc * 2 + yc : rQ ? xc * 8 + yc - 2 : rO ? yc - 10 : 16 + fi;
  const char* wb = rQ ? (const char*)p.wq : rO ? (const char*)p.wo + xc * 524288 : (const char*)p.wmod;
  wb = rM ? (const char*)((lane & 4) ? p.wu : p.wg) + mi * 64 + (lane & 3) * 16 : wb + tile * 128 + (lane & 7) * 16;
  const u32 wstride = rQ ? 8192 : rO ? 1024 : rM ? 3584 : 6144;
  wb += (u >> 3) * 16 * wstride;
  u32x4 w[16];
  u32 epoch = ld4(ws, WS_EPOCH);
  const u32 c0 = ((const u32*)p.c)[u], c1 = ((const u32*)p.c)[256 + u];
  const u32x4 xa = *(const u32x4*)(p.x + lane * 8), xb = *(const u32x4*)(p.x + 512 + lane * 8);
  const int mp = rF ? fi * 8 + (lane & 7) : u;
  const u32 xr0 = ((const u32*)p.x)[mp], xr1 = ((const u32*)p.x)[256 + mp];
  if (rA || rQ) tile_load<PRNT, false>(w, wb, wstride);
  if (u < 32) lds[L_CNT + u] = 0;
  SB();
  wg_barrier();
  SB();
  asm volatile("" : "+v"(epoch));
  const u32 tag = __builtin_amdgcn_readfirstlane(epoch) * 1664525u + 1013904223u;
  u32 ao[4], ao2[4];
#pragma unroll
  for (int v = 0; v < 4; ++v) {
    const bool on = ((lane >> 2) & 3) == v;
    ao[v] = on ? L_ACT + (lane & 1) * 260 + sw * 64 + (lane >> 4) * 16 + (lane & 2) * 4 : L_ZERO;
    ao2[v] = pin(on ? L_ACT2 + sw * 32 + (lane & 1) * 16 + (lane >> 4) * 4 : L_ZERO);
  }
  const u32 wr = pin(L_RED + (lane & 8) * 64 + sw * 128 + (lane & 7) * 16 + (lane >> 4) * 4), rd = pin(L_RED + (lane & 31) * 4 + hf * 2);
  auto modulation = [&]() {
    auto silu = [](float z) { return z / (1.f + __expf(-z)); };
    lds[L_ACT + u] = pk_bf(silu(bf_lo(c0)), silu(bf_hi(c0)));
    lds[L_ACT + 260 + u] = pk_bf(silu(bf_lo(c1)), silu(bf_hi(c1)));
    tile_pack(w);
    tile_mul(w, lds, ao, wr, lane);
    lds_arrive(&lds[L_CNT + 0], lane);
    if (sw == 0) {
      lds_wait(&lds[L_CNT + 0], 4);
      const f32x2 r = tile_red(ldsf, rd);
      st8(ws, WS_MX + tile * 512 + (lane & 31) * 16 + hf * 8, u32x2{pk_bf(r[0], r[1]), tag});
    }
  };
  auto mod = [](float n, float cc, float ss) { return rbf(n * rbf(1.f + cc)) + ss; };

  if (rA) {
    modulation();
  } else if (rQ) {
    float sa_ = 0.f, sb_ = 0.f;
#pragma unroll
    for (int i = 0; i < 4; ++i) { sa_ += bf_lo(xa[i]) + bf_hi(xa[i]); sb_ += bf_lo(xb[i]) + bf_hi(xb[i]); }
    const float ma = wave_sum(sa_) * (1.f / 512), mb = wave_sum(sb_) * (1.f / 512);
    float va = 0.f, vb_ = 0.f;
#pragma unroll
    for (int i = 0; i < 4; ++i) {
      float d0 = bf_lo(xa[i]) - ma, d1 = bf_hi(xa[i]) - ma; va += d0 * d0 + d1 * d1;
      d0 = bf_lo(xb[i]) - mb; d1 = bf_hi(xb[i]) - mb; vb_ += d0 * d0 + d1 * d1;
    }
    const float sda = rbf(sqrtf(rbf(rbf(wave_sum(va) * (1.f / 512)) + 1e-5f))), sdb = rbf(sqrtf(rbf(rbf(wave_sum(vb_) * (1.f / 512)) + 1e-5f)));
    float n0 = rbf(rbf(bf_lo(xr0) - rbf(ma)) / sda), n1 = rbf(rbf(bf_hi(xr0) - rbf(ma)) / sda);
    float n2 = rbf(rbf(bf_lo(xr1) - rbf(mb)) / sdb), n3 = rbf(rbf(bf_hi(xr1) - rbf(mb)) / sdb);
    asm volatile("" : "+v"(n0), "+v"(n1), "+v"(n2), "+v"(n3));
    tile_pack(w);
    __builtin_amdgcn_s_sleep(SLPH);
    u32 m[4];
    gather_tag<2>(ws, [&](int i) { return WS_MX + i * 4096 + u * 16; }, tag, m);
    lds[L_ACT + u] = pk_bf(mod(n0, bf_lo(m[2]), bf_lo(m[0])), mod(n1, bf_hi(m[2]), bf_hi(m[0])));
    lds[L_ACT + 260 + u] = pk_bf(mod(n2, bf_lo(m[3]), bf_lo(m[1])), mod(n3, bf_hi(m[3]), bf_hi(m[1])));
    tile_mul(w, lds, ao, wr, lane);
    lds_arrive(&lds[L_CNT + 0], lane);
    if (sw == 0) {
      lds_wait(&lds[L_CNT + 0], 4);
      const f32x2 r = tile_red(ldsf, rd);
      st8(ws, WS_Q + hf * 16384 + tile * 256 + (lane & 31) * 8, u32x2{pk_bf(r[0], r[1]), tag});
      if (wg == 16 && lane < 16) st8(ws, WS_CLK + (lane >> 1) * 128 + (lane & 1) * 8, u32x2{now(), tag});
    }
  } else {
    __builtin_amdgcn_s_sleep(SLPW);
  }
  if (sw == 0) {
    __builtin_amdgcn_s_sleep(SLPQ);
    u32 qf[6];
    const u32 qo = WS_Q + bb * 16384 + hkv * 2048 + ((lane & 15) >> 2) * 512 + ((lane >> 4) * 4 + (lane & 3)) * 32;
    gather_tag<3, QSL>(ws, [&](int i) { return i == 2 ? WS_CLK + xc * 128 : qo + i * 16; }, tag, qf);
    lds[L_CNT + 10] = qf[4];
    *(u32x4*)&lds[L_Q + lane * 4] = u32x4{qf[0], qf[1], qf[2], qf[3]};
    lds_arrive(&lds[L_CNT + 2], lane);
  } else {
    lds_wait(&lds[L_CNT + 8], 4);
  }
  const u32 o1 = WS_P1 + (rec * 4 + sw) * 640 + (lane == 1 ? 512 : lane * 8), o2 = WS_P2 + xc * 4096 + tile * 512, o4 = WS_P4 + mi * 4096;
  u32x4 wd[8];
  u32 mx[6];
  {
    const u32x4 e4 = {EMPTY, EMPTY, EMPTY, EMPTY};
    if ((lane & 1) == 0 || lane == 1) st16(ws, o1, e4);
    if (rM || (rO && u < 32)) st16(ws, (rM ? o4 : o2) + u * 16, e4);
    drain();
    lds_arrive(&lds[L_CNT + 6], lane);
    if (sw == 0) {
      lds_wait(&lds[L_CNT + 6], 4);
      if (lane == 0) st4(ws, WS_CLR + wg * 4, tag);
    }
  }
  if (rO || rM || rF) {
    tile_load<NRNT, true>(w, wb, wstride);
    if (rF) {
      modulation();
      const u32 t = ((const u32*)p.out)[0];
      asm volatile("" ::"v"(t));
    } else {
      tile_pack(w);
    }
    if (rM) {
      u32x4 r[8];
#pragma unroll
      for (int i = 0; i < 8; ++i) r[i] = ldp<NRNT>((const char*)p.wd + mi * 32768 + i * 1024, (lane >> 4) * 8192 + sw * 256 + (lane & 15) * 16);
      pack8(wd, r);
    }
    __builtin_amdgcn_s_sleep(127);
    if (!rO) gather_tag<3, 32>(ws, [&](int i) { return WS_MX + (i == 1 && rF ? 5 : 2 + i) * 4096 + mp * 16; }, tag, mx);
#pragma unroll 1
    for (u32 spins = 0; spins < SPIN_CAP; ++spins) {
      asm volatile("" ::: "memory");
      const u32x4 c = ld16(ws, WS_CLR + lane * 16);
      if (none(c[0] != tag || c[1] != tag || c[2] != tag || c[3] != tag)) break;
      __builtin_amdgcn_s_sleep(32);
    }
    if (wg == 255 && u == 0) st4(ws, WS_EPOCH, tag);
  }
  {
    wg_barrier();
    const f32x4 mw = *(const f32x4*)&ldsf[L_ML + sw * 8], lw = *(const f32x4*)&ldsf[L_ML + sw * 8 + 4];
    const float M = fmaxf(fmaxf(mw[0], mw[1]), fmaxf(mw[2], mw[3]));
    float L = 0.f, oA = 0.f, oB = 0.f;
#pragma unroll
    for (int w = 0; w < 4; ++w) {
      const float f = __builtin_amdgcn_exp2f((mw[w] - M) * SCL);
      const f32x2 ov = *(const f32x2*)&ldsf[L_O + w * 512 + sw * 128 + 2 * lane];
      L = fmaf(f, lw[w], L); oA = fmaf(f, ov[0], oA); oB = fmaf(f, ov[1], oB);
    }
    const float inv = __builtin_amdgcn_rcpf(L);
    const float fa = __uint_as_float(full(oA * inv)), fb = __uint_as_float(full(oB * inv));
    u32x4 rc = {__float_as_uint(fa), __float_as_uint(fb), __float_as_uint(dpp<XOR1>(fa)), __float_as_uint(dpp<XOR1>(fb))};
    if (lane == 1) rc[0] = full(fmaf(M, SCL, __builtin_amdgcn_logf(L)));
    if ((lane & 1) == 0 || lane == 1) st16(ws, o1, rc);
  }
  if (rO) {
    const u32 rb = WS_P1 + (((hf * 8 + xc) * 16 + pb * 8) * 4 + sw) * 640;
    const u32 aw = pin(L_ACT + hf * 260 + sw * 64 + pb * 32 + (lane & 15) * 2), po = pin(o2 + (lane & 31) * 16 + hf * 8);
    u32x4 d[8][2];
#pragma unroll
    for (int i = 0; i < 8; ++i) asm volatile("" : "=v"(d[i][0]), "=v"(d[i][1]));
    u32 todo = 0xff, miss = 0xff, lsew = EMPTY;
#pragma unroll 1
    for (u32 spins = 0; spins < SPIN_CAP; ++spins) {
      asm volatile("" ::: "memory");
      const u32 go = __builtin_popcount(miss) <= DIRECT ? todo : todo & ~miss;
      if (lsew == EMPTY) lsew = ld4(ws, rb + (lane & 7) * 2560 + 512);
#pragma unroll
      for (int i = 0; i < 8; ++i)
        if (go >> i & 1) { d[i][0] = ld16(ws, rb + (lane & 15) * 16, i * 2560); d[i][1] = ld16(ws, rb + (lane & 15) * 16 + 256, i * 2560); }
      const u64 mm = __builtin_amdgcn_ballot_w64(lsew == EMPTY);
      miss = (u32)mm | (u32)(mm >> 32);
      miss = (miss | miss >> 16) & 0xff;
#pragma unroll
      for (int i = 0; i < 8; ++i)
        if (go >> i & 1) { if (none(has_empty(d[i][0]) || has_empty(d[i][1]))) todo &= ~(1u << i); }
      if ((todo | miss) == 0) break;
      __builtin_amdgcn_s_sleep(SLP1);
    }
    __builtin_amdgcn_s_waitcnt(0x0f70);
    if (u == 0) st4(ws, WS_GO + (xc * 8 + tile) * 128, tag);
    const float lse = __uint_as_float(lsew);
    float M = fmaxf(lse, dpp<XOR1>(lse)); M = fmaxf(M, dpp<XOR2>(M)); M = max16(fmaxf(M, dpp<XOR7>(M)));
    const float e = __builtin_amdgcn_exp2f(lse - M);
    float W = e + dpp<XOR1>(e); W += dpp<XOR2>(W); W += dpp<XOR7>(W);
    const float winv = __builtin_amdgcn_rcpf(fold16(dpp<0x150>(W), dpp<0x150>(W)));
    f32x4 a0, a1;
#define MRG(i) { const float wi = dpp<0x150 + i>(e); const f32x4 t0 = __builtin_bit_cast(f32x4, d[i][0]) * wi, t1 = __builtin_bit_cast(f32x4, d[i][1]) * wi; \
      a0 = i == 0 ? t0 : a0 + t0; a1 = i == 0 ? t1 : a1 + t1; }
    MRG(0) MRG(1) MRG(2) MRG(3) MRG(4) MRG(5) MRG(6) MRG(7)
    *(u32x2*)&lds[aw] =
        u32x2{pk_bf(fold16(a0[0], a1[0]) * winv, fold16(a0[1], a1[1]) * winv), pk_bf(fold16(a0[2], a1[2]) * winv, fold16(a0[3], a1[3]) * winv)};
    tile_mul(w, lds, ao, wr, lane);
    wg_barrier();
    if (sw == 0) {
      const f32x2 r = tile_red(ldsf, rd);
      st8(ws, po, u32x2{full(r[0]), full(r[1])});
    }
    return;
  }
  if (!(rM || (rF && sw == 0))) return;
  const u32 sw_ = pin(L_STAT + (lane >> 4) * 4 + sw), sr = pin(L_STAT + hf * 8), hw = pin((L_ACT2 + sw * 32 + hf * 16 + (lane & 15)) * 2 + pb);
  const u32 po = pin(o4 + sw * 1024 + (lane & 15) * 64 + (lane >> 4) * 16), aw = pin(L_ACT + u);
  f32x2 xi[2] = {up2(xr0), up2(xr1)}, ga[2] = {up2(mx[0]), up2(mx[1])}, sb[2] = {up2(mx[2]), up2(mx[3])};
  f32x2 cb[2] = {rbf2(up2(mx[4]) + 1.f), rbf2(up2(mx[5]) + 1.f)};
#pragma unroll
  for (int b = 0; b < 2; ++b) asm volatile("" : "+v"(xi[b]), "+v"(ga[b]), "+v"(sb[b]), "+v"(cb[b]));
#pragma unroll 1
  for (u32 spins = 0; spins < SPIN_CAP; ++spins) {
    asm volatile("" ::: "memory");
    if (none(ld4(ws, WS_GO + ((lane & 7) * 8 + xc) * 128) != tag)) break;
    __builtin_amdgcn_s_sleep(SLP1);
  }
  __builtin_amdgcn_s_sleep(SLP2);
  if (rM) {
    const f32x4 at = gather_sum<8, 4096>(ws, WS_P2 + u * 16);
    const f32x2 y0 = rbf2(xi[0] + rbf2(ga[0] * rbf2(f32x2{at[0], at[1]}))), y1 = rbf2(xi[1] + rbf2(ga[1] * rbf2(f32x2{at[2], at[3]})));
    {
      const float c0 = __uint_as_float(__builtin_amdgcn_readfirstlane(__float_as_uint(y0[0])));
      const float c1 = __uint_as_float(__builtin_amdgcn_readfirstlane(__float_as_uint(y1[0])));
      const f32x2 d0 = y0 - c0, d1 = y1 - c1;
      const float r = wave_sum4(d0[0] + d0[1], d0[0] * d0[0] + d0[1] * d0[1], d1[0] + d1[1], d1[0] * d1[0] + d1[1] * d1[1]);
      auto t = __builtin_amdgcn_permlane16_swap(__float_as_uint(r), __float_as_uint(r), false, false);
      const float s1 = __uint_as_float(t[0]), s2 = __uint_as_float(t[1]);
      ldsf[sw_] = pb ? fmaf(-s1 * (1.f / 128), s1, s2) : fmaf(s1, 1.f / 128, hf ? c1 : c0);
    }
    wg_barrier();
    const f32x4 m = *(const f32x4*)&ldsf[sr], q = *(const f32x4*)&ldsf[sr + 4];
    const float mu = ((m[0] + m[1]) + (m[2] + m[3])) * 0.25f;
    const f32x4 e = m - mu;
    const float var = ((q[0] + q[1]) + (q[2] + q[3])) * (1.f / 512) + ((e[0] * e[0] + e[1] * e[1]) + (e[2] * e[2] + e[3] * e[3])) * 0.25f;
    const float isd = __builtin_amdgcn_rcpf(rbf(__builtin_amdgcn_sqrtf(rbf(rbf(fmaxf(var, 0.f)) + 1e-5f)))), mr = rbf(mu);
    auto tm = __builtin_amdgcn_permlane32_swap(__float_as_uint(mr), __float_as_uint(mr), false, false);
    auto ti = __builtin_amdgcn_permlane32_swap(__float_as_uint(isd), __float_as_uint(isd), false, false);
    const f32x2 h0 = rbf2(rbf2(rbf2(y0 - __uint_as_float(tm[0])) * __uint_as_float(ti[0])) * cb[0]) + sb[0];
    const f32x2 h1 = rbf2(rbf2(rbf2(y1 - __uint_as_float(tm[1])) * __uint_as_float(ti[1])) * cb[1]) + sb[1];
    lds[aw] = pk_bf(h0[0], h0[1]);
    lds[aw + 260] = pk_bf(h1[0], h1[1]);
    tile_mul(w, lds, ao, wr, lane);
    wg_barrier();
    const f32x2 r = tile_red(ldsf, rd);
    auto t = __builtin_amdgcn_permlane16_swap(__float_as_uint(r[0]), __float_as_uint(r[1]), false, false);
    const float gv = rbf(__uint_as_float(t[0])), uv = rbf(__uint_as_float(t[1]));
    const float inner = 0.7978845608028654f * (gv + 0.044715f * gv * gv * gv);
    const float th = 1.f - 2.f * __builtin_amdgcn_rcpf(1.f + __expf(2.f * inner));
    const float h3 = rbf(0.5f * gv * (1.f + th)) * uv;
    ((__bf16*)lds)[hw] = (__bf16)h3;
    asm volatile("" ::: "memory");
    bf16x8 a[4];
#pragma unroll
    for (int v = 0; v < 4; ++v) a[v] = __builtin_bit_cast(bf16x8, *(const u32x4*)&lds[ao2[v]]);
    SB();
    f32x4 E = {0.f, 0.f, 0.f, 0.f}, O = E;
#pragma unroll
    for (int v = 0; v < 4; ++v) {
      E = __builtin_amdgcn_mfma_f32_16x16x32_bf16(a[v], __builtin_bit_cast(bf16x8, wd[2 * v]), E, 0, 0, 0);
      O = __builtin_amdgcn_mfma_f32_16x16x32_bf16(a[v], __builtin_bit_cast(bf16x8, wd[2 * v + 1]), O, 0, 0, 0);
    }
    st16(ws, po, u32x4{full(E[0]), full(O[0]), full(E[1]), full(O[1])});
  } else {
    const u32 off = (lane >> 3) * 4096 + mp * 16;
    const f32x4 s = gather_sum<1, 0>(ws, WS_P2 + off);
    float at = fold16(fold32(s[0], s[2]), fold32(s[1], s[3]));
    at += dpp<ROR8>(at);
    auto own = [&](const f32x2 (&v)[2]) { const f32x2 t = hf ? v[1] : v[0]; return pb ? t[1] : t[0]; };
    float x2 = rbf(own(xi) + rbf(own(ga) * rbf(at))), gb = own(sb);
    asm volatile("" : "+v"(x2), "+v"(gb));
    __builtin_amdgcn_s_sleep(SLP3);
    const f32x4 dv = gather_sum<7, 8 * 4096>(ws, WS_P4 + off);
    float dn = fold16(fold32(dv[0], dv[2]), fold32(dv[1], dv[3]));
    dn += dpp<ROR8>(dn);
    const float res = x2 + rbf(gb * rbf(dn));
    if ((lane & 8) == 0) ((__bf16*)p.out)[hf * 512 + mp * 2 + pb] = (__bf16)res;
  }
}

extern "C" void kernel_launch(void* const* d_in, const int* in_sizes, int n_in, void* d_out, int out_size, void* d_ws,
                              size_t ws_size, hipStream_t stream) {
  Params p;
  p.x = (const u16*)d_in[0];
  p.k = (const u16*)d_in[1];
  p.v = (const u16*)d_in[2];
  p.wq = (const u16*)d_in[3];
  p.wo = (const u16*)d_in[4];
  p.wg = (const u16*)d_in[6];
  p.wu = (const u16*)d_in[7];
  p.wd = (const u16*)d_in[8];
  p.c = (const u16*)d_in[9];
  p.wmod = (const u16*)d_in[10];
  p.out = (u16*)d_out;
  p.ws = (char*)d_ws;
  fused_block_kernel<<<NWG, NTHR, 0, stream>>>(p);
}
```
